```python
import torch

def _softmax(x, axis=-1):
    return torch.softmax(x, dim=axis)
def _gelu(x):
    return torch.nn.functional.gelu(x, approximate="tanh")
def _silu(x):
    return torch.nn.functional.silu(x)
def _sigmoid(x):
    return torch.sigmoid(x)
def _layernorm(x, axis=-1, eps=1e-5):
    m = x.mean(dim=axis, keepdim=True)
    v = x.var(dim=axis, keepdim=True, unbiased=False)
    return (x - m) / torch.sqrt(v + eps)
def _scatter_add(tgt, idx, src, axis=0):
    return tgt.clone().index_add_(axis, idx, src)
def _segment_sum(src, seg, n):
    out = torch.zeros((n,) + src.shape[1:], dtype=src.dtype)
    return out.index_add_(0, seg, src)
def _pad(x, pw):
    flat = tuple(v for pair in reversed(pw) for v in pair)
    return torch.nn.functional.pad(x, flat)

kernel_name = 'interleave_layers__sliding_local_dilated__swiglu_ffn__prompt'

def setup_inputs():

    _w = (lambda a: (lambda t: t.to(torch.bfloat16) if t.is_floating_point() else t)(torch.as_tensor(a)))
    def _A_setup():
        B, S, D = 1, 2048, 448
        return {
            "x": _w((lambda *s: torch.randn(*s, dtype=torch.bfloat16) * 0.05)(B, S, D)),
            "wqkv": _w((lambda *s: torch.randn(*s, dtype=torch.bfloat16) * 0.05)(D, 3 * D)),
            "wo": _w((lambda *s: torch.randn(*s, dtype=torch.bfloat16) * 0.05)(D, D)),
        }
    _A_in = _A_setup()

    def _B_setup():
        B, S, D, F = 1, 2048, 448, 1792
        return {
            "x": _w((lambda *s: torch.randn(*s, dtype=torch.bfloat16) * 0.05)(B, S, D)),
            "w_gate": _w((lambda *s: torch.randn(*s, dtype=torch.bfloat16) * 0.05)(D, F)),
            "w_up": _w((lambda *s: torch.randn(*s, dtype=torch.bfloat16) * 0.05)(D, F)),
            "w_down": _w((lambda *s: torch.randn(*s, dtype=torch.bfloat16) * 0.05)(F, D)),
        }
    _B_in = _B_setup()

    return {
        "A_x": _A_in["x"],
        "A_wqkv": _A_in["wqkv"],
        "A_wo": _A_in["wo"],
        "B_x": _B_in["x"],
        "B_w_gate": _B_in["w_gate"],
        "B_w_up": _B_in["w_up"],
        "B_w_down": _B_in["w_down"],
    }

def reference(A_x, A_wqkv, A_wo, B_x, B_w_gate, B_w_up, B_w_down):
    def _A_ref(x, wqkv, wo):
        B, S, D = x.shape
        H = 16; Dh = D // H
        W, dil = 8, 2
        qkv = (lambda x, *d: x.permute(*d))((x @ wqkv).reshape(B, S, 3, H, Dh), 2, 0, 3, 1, 4)
        q, k, v = qkv[0], qkv[1], qkv[2]
        scale = Dh ** -0.5
        pos = torch.arange(S)
        off = torch.arange(W) * dil
        idx = pos[:, None] - off[None, :]
        mask = idx >= 0
        idx_c = torch.where(mask, idx, 0).reshape(-1)
        kg = k[:, :, idx_c, :].reshape(B, H, S, W, Dh)
        vg = v[:, :, idx_c, :].reshape(B, H, S, W, Dh)
        logits = torch.einsum("bhsd,bhswd->bhsw", q, kg) * scale
        logits = torch.where(mask[None, None, :, :], logits, -1e9)
        attn = _softmax(logits, axis=-1)
        o = torch.einsum("bhsw,bhswd->bhsd", attn, vg)
        return (lambda x, *d: x.permute(*d))(o, 0, 2, 1, 3).reshape(B, S, D) @ wo

    def _B_ref(x, w_gate, w_up, w_down):
        g = _silu(x @ w_gate)
        u = x @ w_up
        return (g * u) @ w_down

    x = A_x
    x = x + _A_ref(_layernorm(x, axis=-1), A_wqkv, A_wo)
    x = x + _B_ref(_layernorm(x, axis=-1), B_w_gate, B_w_up, B_w_down)
    x = x + _A_ref(_layernorm(x, axis=-1), A_wqkv, A_wo)
    x = x + _B_ref(_layernorm(x, axis=-1), B_w_gate, B_w_up, B_w_down)
    return x
```

```cpp
#include <hip/hip_runtime.h>
#include <hip/hip_bf16.h>
#include <utility>

typedef __attribute__((ext_vector_type(8))) __bf16 bf16x8;
typedef __attribute__((ext_vector_type(2))) __bf16 bf16x2;
typedef __attribute__((ext_vector_type(16))) float f32x16;
typedef __attribute__((ext_vector_type(4))) float f32x4;
typedef __attribute__((ext_vector_type(2))) float f32x2;
typedef __attribute__((ext_vector_type(4))) unsigned u32x4;
typedef __attribute__((ext_vector_type(2))) unsigned u32x2;
typedef __amdgpu_buffer_rsrc_t rsrc_t;
#define DI __device__ __forceinline__
#define LDSP __attribute__((address_space(3)))

constexpr int S_ = 2048, D_ = 448, F_ = 1792;
constexpr unsigned KB = 1024;
constexpr unsigned OFF_FLAG = 0;
constexpr unsigned OFF_CS   = 32 * KB;
constexpr unsigned OFF_WQKV = 64 * KB;
constexpr unsigned OFF_WO   = OFF_WQKV + 1344 * KB;
constexpr unsigned OFF_WGU  = OFF_WO + 448 * KB;
constexpr unsigned OFF_WD   = OFF_WGU + 3136 * KB;
constexpr unsigned OFF_X    = OFF_WD + 1568 * KB;
constexpr unsigned X_STAT   = 1792 * KB;
constexpr unsigned X_SZ     = 2048 * KB;
constexpr unsigned OFF_O    = OFF_X + 4 * X_SZ;
constexpr unsigned O_SZ     = 2048 * KB;
constexpr unsigned OFF_H    = OFF_O + 2 * O_SZ;
constexpr unsigned H_SZ     = 7168 * KB;
constexpr unsigned OFF_DBG  = OFF_H + 2 * H_SZ;
constexpr unsigned WS_USED  = OFF_DBG + 64 * KB;
constexpr unsigned SLOT = 28 * KB;
constexpr unsigned LDS_CS = 5 * SLOT;
constexpr unsigned LDS_BYTES = LDS_CS + 2 * KB;
constexpr unsigned KV_HALO = 2 * 4 * 2 * 144 * 16;
constexpr int SC1 = 16;

DI unsigned pack_bf16(float a, float b) {
  bf16x2 v; v[0] = (__bf16)a; v[1] = (__bf16)b;
  return __builtin_bit_cast(unsigned, v);
}
DI float bf_lo(unsigned u) { return __builtin_bit_cast(float, u << 16); }
DI float bf_hi(unsigned u) { return __builtin_bit_cast(float, u & 0xffff0000u); }
DI float xor32_sum(float s) {
  unsigned a = __builtin_bit_cast(unsigned, s);
  auto r = __builtin_amdgcn_permlane32_swap(a, a, false, false);
  return __builtin_bit_cast(float, (unsigned)r[0]) + __builtin_bit_cast(float, (unsigned)r[1]);
}

DI int frag_row(int i, int hf) { return (i & 3) + 8 * (i >> 2) + 4 * hf; }

DI void load8x8(const __hip_bfloat16* src, int ld, int nv, u32x4 (&out)[8]) {
  u32x4 in[8];
#pragma unroll
  for (int r = 0; r < 8; ++r) {
    u32x4 z = {0u, 0u, 0u, 0u};
    in[r] = (r < nv) ? *(const u32x4*)(src + (size_t)r * ld) : z;
  }
#pragma unroll
  for (int c = 0; c < 8; ++c)
#pragma unroll
    for (int q = 0; q < 4; ++q)
      out[c][q] = __builtin_amdgcn_perm(in[2 * q + 1][c >> 1], in[2 * q][c >> 1], (c & 1) ? 0x07060302u : 0x05040100u);
}

__global__ void __launch_bounds__(256) prep_kernel(const __hip_bfloat16* __restrict__ x, const __hip_bfloat16* __restrict__ wqkv,
                                                   const __hip_bfloat16* __restrict__ wo, const __hip_bfloat16* __restrict__ wg,
                                                   const __hip_bfloat16* __restrict__ wu, const __hip_bfloat16* __restrict__ wd,
                                                   char* __restrict__ ws) {
  __shared__ float red[56][33];
  const int t = threadIdx.x;
  int b = blockIdx.x;
  if (b < 154) {
    const int kind = b < 42 ? 0 : (b < 98 ? 1 : 2);
    const int cb = kind == 0 ? b : (kind == 1 ? b - 42 : b - 98);
    const __hip_bfloat16* w = kind == 0 ? wqkv : (kind == 1 ? wg : wu);
    const int ld = kind == 0 ? 3 * D_ : F_;
    const int rg = t >> 2, c0 = cb * 32 + (t & 3) * 8;
    if (t < 224) {
      u32x4 o[8];
      load8x8(w + (size_t)rg * 8 * ld + c0, ld, 8, o);
      const int ks = rg >> 1, hfa = rg & 1;
#pragma unroll
      for (int c = 0; c < 8; ++c) {
        const int col = c0 + c;
        unsigned dst;
        if (kind == 0) {
          const int g = col / D_, rem = col - g * D_, h = rem / 28, d = rem - h * 28, hf = d / 14, i = d - hf * 14;
          dst = OFF_WQKV + ((h * 3 + g) * 28 + ks) * KB + (32 * hfa + frag_row(i, hf)) * 16;
        } else {
          const int grp = col >> 4, hf = (col >> 3) & 1, i = (col & 7) + (kind == 2 ? 8 : 0);
          dst = OFF_WGU + (grp * 28 + ks) * KB + (32 * hfa + frag_row(i, hf)) * 16;
        }
        *(u32x4*)(ws + dst) = o[c];
        float s = 0.f;
#pragma unroll
        for (int q = 0; q < 4; ++q) s += bf_lo(o[c][q]) + bf_hi(o[c][q]);
        red[rg][(t & 3) * 8 + c] = s;
      }
    }
    if (kind == 0) {
      const int e = b * 256 + t, fr = e >> 3, hfa = (e >> 2) & 1, hf = (e >> 1) & 1, i = 14 + (e & 1);
      u32x4 z = {0u, 0u, 0u, 0u};
      *(u32x4*)(ws + OFF_WQKV + fr * KB + (32 * hfa + frag_row(i, hf)) * 16) = z;
    }
    __syncthreads();
    if (t < 32) {
      float s = 0.f;
#pragma unroll 8
      for (int r = 0; r < 56; ++r) s += red[r][t];
      const int col = cb * 32 + t;
      float* cs = (float*)(ws + OFF_CS);
      if (kind == 0) {
        const int g = col / D_, rem = col - g * D_, h = rem / 28, d = rem - h * 28, hf = d / 14, i = d - hf * 14;
        cs[(h * 10 + g) * 32 + hf * 16 + i] = s;
      } else {
        const int grp = col >> 4, jc = grp / 7, g = grp - jc * 7, hf = (col >> 3) & 1, i = (col & 7) + (kind == 2 ? 8 : 0);
        cs[(jc * 10 + 3 + g) * 32 + hf * 16 + i] = s;
      }
    }
    return;
  }
  b -= 154;
  if (b < 63) {
    const bool is_o = b < 14;
    const int e = (is_o ? b : b - 14) * 256 + t, rg = e / 56, cc = e - rg * 56;
    const int ks = rg >> 1, hfa = rg & 1;
    int row0, nv; unsigned base; int nks;
    if (is_o) { const int h = ks >> 1, u = ks & 1; row0 = h * 28 + 14 * hfa + 8 * u; nv = u ? 6 : 8; base = OFF_WO; nks = 32; }
    else { row0 = rg * 8; nv = 8; base = OFF_WD; nks = 112; }
    u32x4 o[8];
    load8x8((is_o ? wo : wd) + (size_t)row0 * D_ + cc * 8, D_, nv, o);
    const int tile = cc >> 2, hf = cc & 1, ib = ((cc >> 1) & 1) * 8;
#pragma unroll
    for (int c = 0; c < 8; ++c)
      *(u32x4*)(ws + base + (tile * nks + ks) * KB + (32 * hfa + frag_row(ib + c, hf)) * 16) = o[c];
    return;
  }
  b -= 63;
  if (b < 64) {
    u32x4 v[7];
#pragma unroll
    for (int q = 0; q < 7; ++q) v[q] = *(const u32x4*)(x + (size_t)b * 32 * D_ + (t + 256 * q) * 8);
#pragma unroll
    for (int q = 0; q < 7; ++q) {
      const int e = t + 256 * q, tok = e / 56, ch = e - tok * 56;
      *(u32x4*)(ws + OFF_X + (b * 28 + (ch >> 1)) * KB + (32 * (ch & 1) + tok) * 16) = v[q];
      float s = 0.f, ss = 0.f;
#pragma unroll
      for (int k = 0; k < 4; ++k) { const float lo = bf_lo(v[q][k]), hi = bf_hi(v[q][k]); s += lo + hi; ss += lo * lo + hi * hi; }
      s += __shfl_xor(s, 1); ss += __shfl_xor(ss, 1);
      s += __shfl_xor(s, 2); ss += __shfl_xor(ss, 2);
      if ((t & 3) == 0) { f32x2 st = {s, ss}; *(f32x2*)(ws + OFF_X + X_STAT + ((b * 14 + (ch >> 2)) * 32 + tok) * 8) = st; }
    }
    return;
  }
  u32x4 z = {0u, 0u, 0u, 0u};
#pragma unroll
  for (int q = 0; q < 8; ++q) *(u32x4*)(ws + OFF_FLAG + (t + 256 * q) * 16) = z;
  if (t < 192) ((float*)(ws + OFF_CS))[((t / 12) * 10 + (t / 4) % 3) * 32 + ((t >> 1) & 1) * 16 + 14 + (t & 1)] = 0.f;
}

#define BARRIER() asm volatile("s_waitcnt lgkmcnt(0)\n\ts_barrier" ::: "memory")
#define RAW_BARRIER() asm volatile("s_barrier" ::: "memory")
#define VMCNT(n) asm volatile("s_waitcnt vmcnt(" #n ")" ::: "memory")
#define FENCE() asm volatile("" ::: "memory")
#define SCHED() __builtin_amdgcn_sched_barrier(0)
#define PIN(x) asm volatile("" : "+v"(x))
#define STAMP(i) do {} while (0)

struct Ctx {
  rsrc_t rs; unsigned l16, tid; int lane, w, p, j, tok, hf;
  LDSP char* lds;
};

template <int AUX>
DI bf16x8 ld_frag(const Ctx& c, unsigned off) {
  return __builtin_bit_cast(bf16x8, __builtin_amdgcn_raw_buffer_load_b128(c.rs, c.l16, off, AUX));
}
DI void ld_frag_agpr(const Ctx& c, bf16x8& dst, unsigned off) {
  asm volatile("buffer_load_dwordx4 %0, %1, %2, %3 offen" : "=a"(dst) : "v"(c.l16), "s"(c.rs), "s"(off) : "memory");
}
DI void relaunder(Ctx& c) {
  asm volatile("" : "+v"(c.tid));
  asm volatile("" : "+s"(c.w), "+s"(c.p), "+s"(c.j));
  c.lane = c.tid & 63; c.l16 = c.lane * 16u; c.tok = c.lane & 31; c.hf = c.lane >> 5;
}
DI LDSP char* lds_base(const Ctx& c, unsigned off) {
  unsigned v = off; PIN(v);
  return c.lds + v;
}
template <bool FIRST> DI void mfma_v(f32x16& acc, const bf16x8& a, const bf16x8& b) {
  if (FIRST) asm volatile("v_mfma_f32_32x32x16_bf16 %0, %1, %2, 0" : "=&v"(acc) : "v"(a), "v"(b));
  else asm volatile("v_mfma_f32_32x32x16_bf16 %0, %1, %2, %0" : "+v"(acc) : "v"(a), "v"(b));
}
template <bool FIRST> DI void mfma_a(f32x16& acc, const bf16x8& a, const bf16x8& b) {
  if (FIRST) asm volatile("v_mfma_f32_32x32x16_bf16 %0, %1, %2, 0" : "=&v"(acc) : "a"(a), "v"(b));
  else asm volatile("v_mfma_f32_32x32x16_bf16 %0, %1, %2, %0" : "+v"(acc) : "a"(a), "v"(b));
}
DI void mfma_done(f32x16& acc) { asm volatile("s_nop 13" : "+v"(acc)); }

DI void dma_frag(const Ctx& c, unsigned src, unsigned slot, int i) {
  unsigned keep;
  asm volatile("s_mov_b32 %0, m0\n\ts_mov_b32 m0, %3\n\ts_nop 0\n\tbuffer_load_dwordx4 %1, %2, %4 offen lds\n\ts_mov_b32 m0, %0"
               : "=&s"(keep) : "v"(c.l16), "s"(c.rs), "s"(slot * SLOT + (c.w * 7 + i) * KB), "s"(src + (c.w * 7 + i) * KB) : "memory");
}
DI void dma_group(const Ctx& c, unsigned src, unsigned slot) {
  const unsigned s0 = src + c.w * 7 * KB, m0 = slot * SLOT + c.w * 7 * KB;
  unsigned keep;
  asm volatile(
      "s_mov_b32 %0, m0\n\ts_mov_b32 m0, %3\n\ts_nop 4\n\t"
      "buffer_load_dwordx4 %1, %2, %5 offen lds\n\t"
      "buffer_load_dwordx4 %1, %2, %5 offen offset:1024 lds\n\t"
      "buffer_load_dwordx4 %1, %2, %5 offen offset:2048 lds\n\t"
      "buffer_load_dwordx4 %1, %2, %5 offen offset:3072 lds\n\t"
      "s_mov_b32 m0, %4\n\ts_nop 0\n\t"
      "buffer_load_dwordx4 %1, %2, %6 offen lds\n\t"
      "buffer_load_dwordx4 %1, %2, %6 offen offset:1024 lds\n\t"
      "buffer_load_dwordx4 %1, %2, %6 offen offset:2048 lds\n\t"
      "s_mov_b32 m0, %0"
      : "=&s"(keep) : "v"(c.l16), "s"(c.rs), "s"(m0), "s"(m0 + 4 * KB), "s"(s0), "s"(s0 + 4 * KB) : "memory");
}

DI void wait_flags(const Ctx& c, int word) {
#pragma unroll 1
  for (int spin = 0; spin < (1 << 20); ++spin) {
    const unsigned v = __builtin_amdgcn_raw_buffer_load_b32(c.rs, (unsigned)word * 4u, OFF_FLAG, SC1);
    if (__all(v != 0)) break;
    __builtin_amdgcn_s_sleep(1);
    FENCE();
  }
  FENCE();
}
DI void set_flag(const Ctx& c, int stage) {
  if (c.tid == 0) __builtin_amdgcn_raw_buffer_store_b32(1u, c.rs, (unsigned)((stage * 16 + c.p) * 64 + c.j) * 4u, OFF_FLAG, SC1);
  FENCE();
}
DI void finish_stats(float s, float ss, float& mean, float& rstd) {
  mean = s * (1.0f / D_);
  const float var = fmaxf(ss * (1.0f / D_) - mean * mean, 0.f);
  rstd = __builtin_amdgcn_rsqf(var + 1e-5f);
}
template <int AUX>
DI void load_tokens(const Ctx& c, unsigned xoff, bf16x8 (&B)[28], f32x2 (&st)[7]) {
  const unsigned tf = c.p * 4 + c.w;
#pragma unroll
  for (int q = 0; q < 7; ++q)
    st[q] = __builtin_bit_cast(f32x2, __builtin_amdgcn_raw_buffer_load_b64(c.rs, c.lane * 8u, xoff + X_STAT + (tf * 14 + 2 * q) * 256, AUX));
#pragma unroll
  for (int ks = 0; ks < 28; ++ks) B[ks] = ld_frag<AUX>(c, xoff + (tf * 28 + ks) * KB);
  FENCE();
}
DI void token_stats(const f32x2 (&st)[7], float& mean, float& rstd) {
  float s = 0.f, ss = 0.f;
#pragma unroll
  for (int q = 0; q < 7; ++q) { s += st[q][0]; ss += st[q][1]; }
  finish_stats(xor32_sum(s), xor32_sum(ss), mean, rstd);
  PIN(mean); PIN(rstd);
}

template <int B0, class F, int... I>
DI void static_for_impl(F&& f, std::integer_sequence<int, I...>) { (f(std::integral_constant<int, B0 + I>{}), ...); }
template <int B0, int E0, class F>
DI void static_for(F&& f) { static_for_impl<B0>(f, std::make_integer_sequence<int, (E0 > B0 ? E0 - B0 : 0)>{}); }
template <int NP> constexpr int st_grp(int s) { return s < NP * 28 ? (s / 56) * 2 + (s & 1) : s / 28; }
template <int NP> constexpr int st_ks(int s) { return s < NP * 28 ? (s % 56) >> 1 : s % 28; }
template <int NG, int NP, class Fill>
DI void gemm_stream(LDSP char* const (&base)[NG], const bf16x8 (&B)[28], f32x16 (&acc)[NG], Fill fill) {
  constexpr int PD = 4, NS = NG * 28;
  bf16x8 a[NS];
#pragma unroll
  for (int s = 0; s < PD; ++s) a[s] = *(LDSP bf16x8*)(base[st_grp<NP>(s)] + st_ks<NP>(s) * KB);
  static_for<0, NS>([&](auto S) __attribute__((always_inline)) {
    constexpr int s = S;
    SCHED();
    mfma_v<st_ks<NP>(s) == 0>(acc[st_grp<NP>(s)], a[s], B[st_ks<NP>(s)]);
    SCHED();
    if constexpr (s + PD < NS) a[s + PD] = *(LDSP bf16x8*)(base[st_grp<NP>(s + PD)] + st_ks<NP>(s + PD) * KB);
    fill(S);
  });
  SCHED();
}

template <int L>
DI void stage_qkv(Ctx& c, bf16x8 (&Wo)[16], bf16x8 (&Wd)[56], unsigned (&xres)[8]) {
  constexpr int AUX = L ? SC1 : 0;
  constexpr unsigned SB = L ? 2 : 0, SCR = (L ? 0 : 3) * SLOT;
  constexpr unsigned xoff = OFF_X + (L ? 2 : 0) * X_SZ;
  const int jj = min(c.j, 13), th = jj / 7, fc = jj - th * 7;
  if (L) {
    const int l7 = c.lane % 7;
    wait_flags(c, (c.lane < 32 || c.p == 0) ? (3 * 16 + c.p) * 64 + 7 * (c.w >> 1) + l7 : (3 * 16 + c.p - 1) * 64 + 7 + l7);
  }
  STAMP(L * 16 + 1);
  u32x4 hx[4]; f32x2 hst[4];
  const unsigned ptf = c.p > 0 ? c.p * 4 - 1 : c.w;
#pragma unroll
  for (int q = 0; q < 4; ++q) {
    const unsigned e = min(c.tid + 256u * q, 895u);
    hx[q] = __builtin_amdgcn_raw_buffer_load_b128(c.rs, (e >> 4) * 512 + (16 + (e & 15)) * 16, xoff + ptf * 28 * KB, AUX);
    const unsigned tile = min((c.lane >> 4) + 4u * q, 13u);
    hst[q] = __builtin_bit_cast(f32x2, __builtin_amdgcn_raw_buffer_load_b64(c.rs, tile * 256 + (16 + (c.lane & 15)) * 8, xoff + X_STAT + ptf * 14 * 256, AUX));
  }
  bf16x8 B[28]; f32x2 st[7];
  load_tokens<AUX>(c, xoff, B, st);
  if (L == 0) {
#pragma unroll
    for (int u = 0; u < 2; ++u) {
      u32x4 v = __builtin_amdgcn_raw_buffer_load_b128(c.rs, c.l16, xoff + ((c.p * 4 + 2 * th + (c.w >> 1)) * 28 + 4 * fc + 2 * (c.w & 1) + u) * KB, 0);
#pragma unroll
      for (int q = 0; q < 4; ++q) xres[4 * u + q] = v[q];
    }
    FENCE();
    VMCNT(45);
    BARRIER();
  }
  LDSP char* const wb[3] = {lds_base(c, (SB + 1) * SLOT + c.l16), lds_base(c, (SB + 2) * SLOT + c.l16), lds_base(c, SB * SLOT + c.l16)};
  LDSP char* const csb = lds_base(c, LDS_CS + c.hf * 64);
  LDSP char* const kvw = lds_base(c, SCR + (c.hf * 144 + 16 + 32 * c.w + c.tok) * 16);
  const int kv = c.w >> 1, rh = c.w & 1, hq = (c.lane >> 4) & 1, ch = 2 * rh + (c.lane >> 5);
  LDSP char* const ha = lds_base(c, (SB + 1 + kv) * SLOT + (c.lane >> 5) * KB + (32 * hq + 16 * rh + (c.lane & 15)) * 16);
  LDSP char* const hb = lds_base(c, SCR + KV_HALO + c.l16);
  f32x16 acc[3];
  f32x4 a4, cs4; bf16x8 hfa[14], hfb[14];
  float mean, rstd, hm, hr;
  auto norm4 = [&](const f32x4& v, const f32x4& cs, float m, float r) {
    f32x4 o;
#pragma unroll
    for (int e = 0; e < 4; e += 2) {
      const unsigned pk = pack_bf16((v[e] - m * cs[e]) * r, (v[e + 1] - m * cs[e + 1]) * r);
      o[e] = bf_lo(pk); o[e + 1] = bf_hi(pk);
    }
    return o;
  };
  gemm_stream<3, 2>(wb, B, acc, [&](auto S) __attribute__((always_inline)) {
    constexpr int s = S;
    if constexpr (s >= 2 && s < 6) { if (s < 5 || c.tid < 128) *(LDSP u32x4*)(hb + c.w * KB + (s - 2) * 4 * KB) = hx[s - 2]; }
    if constexpr (s == 6) token_stats(st, mean, rstd);
    if constexpr (s == 8) {
      float t = hst[0][0] + hst[1][0] + hst[2][0], tt = hst[0][1] + hst[1][1] + hst[2][1];
      if (c.lane < 32) { t += hst[3][0]; tt += hst[3][1]; }
      t += __shfl_xor(t, 16); tt += __shfl_xor(tt, 16);
      finish_stats(xor32_sum(t), xor32_sum(tt), hm, hr);
      PIN(hm); PIN(hr);
    }
    if constexpr (s == 20) RAW_BARRIER();
    if constexpr (s >= 22 && s < 36) { hfa[s - 22] = *(LDSP bf16x8*)(ha + (s - 22) * 2 * KB); hfb[s - 22] = *(LDSP bf16x8*)(hb + (s - 22) * KB); }
    if constexpr (s >= 25 && s < 39) {
      if constexpr (s == 25) asm volatile("v_mfma_f32_16x16x32_bf16 %0, %1, %2, 0" : "=&v"(a4) : "v"(hfa[0]), "v"(hfb[0]));
      else asm volatile("v_mfma_f32_16x16x32_bf16 %0, %1, %2, %0" : "+v"(a4) : "v"(hfa[s - 25]), "v"(hfb[s - 25]));
    }
    if constexpr (s == 40) cs4 = *(LDSP f32x4*)(c.lds + LDS_CS + ((1 + kv) * 32 + hq * 16 + ch * 4) * 4);
    if constexpr (s == 46) {
      asm volatile("" : "+v"(a4));
      f32x4 r = norm4(a4, cs4, hm, hr);
      if (c.p == 0) r = f32x4{0.f, 0.f, 0.f, 0.f};
      *(LDSP f32x4*)(c.lds + SCR + (((kv * 4 + ch) * 2 + hq) * 144 + (c.lane & 15)) * 16) = r;
    }
    if constexpr (L == 0 && s >= 28 && s < 56) { const int i = s - 28; ld_frag_agpr(c, Wd[i], OFF_WD + ((fc * 2 + (i & 1)) * 112 + c.w * 28 + (i >> 1)) * KB); }
    if constexpr (s >= 59 && s < 83) {
      constexpr int u = (s - 59) / 3, g = u >> 2, k4 = u & 3;
      if ((s - 59) % 3 == 0) cs4 = *(LDSP f32x4*)(csb + ((1 + g) * 32 + k4 * 4) * 4);
      if ((s - 59) % 3 == 2) {
        if (k4 == 0) PIN(acc[g]);
        const f32x4 v = {acc[g][4 * k4], acc[g][4 * k4 + 1], acc[g][4 * k4 + 2], acc[g][4 * k4 + 3]};
        *(LDSP f32x4*)(kvw + (g * 4 + k4) * 288 * 16) = norm4(v, cs4, mean, rstd);
      }
    }
    if constexpr (s >= 64 && s < 80) { const int i = s - 64; Wo[i] = ld_frag<0>(c, OFF_WO + ((fc * 2 + (i >> 3)) * 32 + c.w * 8 + (i & 7)) * KB); }
  });
  STAMP(L * 16 + 2);
  f32x4 csq[4];
#pragma unroll
  for (int k4 = 0; k4 < 4; ++k4) csq[k4] = *(LDSP f32x4*)(csb + k4 * 16);
  BARRIER();
  LDSP char* const rb = kvw - 14 * 16;
  const int pos = c.p * 128 + c.w * 32 + c.tok;
  float lg[8], mx = -3.0e38f, den = 0.f, o[16];
#pragma unroll
  for (int i = 0; i < 16; ++i) o[i] = 0.f;
  f32x4 kvb[16][4];
  auto kv_read = [&](int s) {
#pragma unroll
    for (int k4 = 0; k4 < 4; ++k4) kvb[s][k4] = *(LDSP f32x4*)(rb + (((s >> 3) * 4 + k4) * 288 + 14 - 2 * (s & 7)) * 16);
  };
  kv_read(0); kv_read(1); kv_read(2);
  mfma_done(acc[2]);
  f32x4 qv[4];
#pragma unroll
  for (int k4 = 0; k4 < 4; ++k4) {
    const f32x4 v = {acc[2][4 * k4], acc[2][4 * k4 + 1], acc[2][4 * k4 + 2], acc[2][4 * k4 + 3]};
    qv[k4] = norm4(v, csq[k4], mean, rstd);
  }
#pragma unroll
  for (int s = 0; s < 16; ++s) {
    SCHED();
    if (s + 3 < 16) kv_read(s + 3);
    if (s < 8) {
      float d0 = 0.f, d1 = 0.f;
#pragma unroll
      for (int i = 0; i < 14; i += 2) { d0 = fmaf(qv[i >> 2][i & 3], kvb[s][i >> 2][i & 3], d0); d1 = fmaf(qv[i >> 2][(i & 3) + 1], kvb[s][i >> 2][(i & 3) + 1], d1); }
      const float d = xor32_sum(d0 + d1) * 0.18898223650461361f;
      lg[s] = (pos - 2 * s >= 0) ? d : -1e9f;
      mx = fmaxf(mx, lg[s]);
    } else {
      const float pw = __builtin_amdgcn_exp2f((lg[s - 8] - mx) * 1.4426950408889634f);
      den += pw;
#pragma unroll
      for (int i = 0; i < 14; ++i) o[i] = fmaf(pw, kvb[s][i >> 2][i & 3], o[i]);
    }
  }
  SCHED();
  const float inv = __builtin_amdgcn_rcpf(den);
#pragma unroll
  for (int u = 0; u < 2; ++u) {
    u32x4 pk;
#pragma unroll
    for (int q = 0; q < 4; ++q) pk[q] = pack_bf16(o[8 * u + 2 * q] * inv, o[8 * u + 2 * q + 1] * inv);
    __builtin_amdgcn_raw_buffer_store_b128(pk, c.rs, c.l16, OFF_O + L * O_SZ + ((c.p * 4 + c.w) * 32 + 2 * c.j + u) * KB, SC1);
  }
  FENCE();
  STAMP(L * 16 + 3);
#pragma unroll
  for (int g = 0; g < 3; ++g) dma_group(c, OFF_WGU + (c.j * 7 + g) * SLOT, SB + g);
  VMCNT(21);
  BARRIER();
  set_flag(c, L * 4 + 0);
  STAMP(L * 16 + 4);
}

template <int L, bool DOWN, int NW>
DI void stage_proj(Ctx& c, const bf16x8 (&W)[NW], unsigned (&xres)[8], __hip_bfloat16* out) {
  constexpr int KW = NW / 2, KS = 4 * KW, WIN = DOWN ? 20 : 8;
  constexpr unsigned SCR = (DOWN || L ? 0 : 3) * SLOT;
  constexpr unsigned aoff = DOWN ? OFF_H + L * H_SZ : OFF_O + L * O_SZ;
  constexpr int SI = L * 16 + (DOWN ? 12 : 5);
  if (c.j < 14) {
    const int th = c.j / 7, fc = c.j - th * 7;
    wait_flags(c, ((L * 4 + (DOWN ? 2 : 0)) * 16 + c.p) * 64 + 4 * c.w + (c.lane & 3));
    STAMP(SI);
    const unsigned a0 = aoff + ((c.p * 4 + 2 * th) * KS + c.w * KW) * KB;
    bf16x8 B[KW][2];
#pragma unroll
    for (int k = 0; k < WIN; ++k) { B[k][0] = ld_frag<SC1>(c, a0 + k * KB); B[k][1] = ld_frag<SC1>(c, a0 + (KS + k) * KB); }
    f32x16 acc[4];
#pragma unroll
    for (int k = 0; k < KW; ++k) {
      SCHED();
#pragma unroll
      for (int m = 0; m < 4; ++m) {
        if (DOWN) { if (k == 0) mfma_a<true>(acc[m], W[2 * k + (m & 1)], B[k][m >> 1]); else mfma_a<false>(acc[m], W[2 * k + (m & 1)], B[k][m >> 1]); }
        else { if (k == 0) mfma_v<true>(acc[m], W[(m & 1) * KW + k], B[k][m >> 1]); else mfma_v<false>(acc[m], W[(m & 1) * KW + k], B[k][m >> 1]); }
      }
      SCHED();
      if (k + WIN < KW) { B[k + WIN][0] = ld_frag<SC1>(c, a0 + (k + WIN) * KB); B[k + WIN][1] = ld_frag<SC1>(c, a0 + (KS + k + WIN) * KB); }
    }
    asm volatile("s_nop 13" : "+v"(acc[0]), "+v"(acc[1]), "+v"(acc[2]), "+v"(acc[3]));
    STAMP(SI + 1);
    LDSP char* const sb = lds_base(c, SCR + c.l16);
    f32x16 r;
#pragma unroll
    for (int tl = 0; tl < 4; ++tl) {
      if (tl != c.w) {
        const unsigned src = (c.w - tl - 1) & 3;
#pragma unroll
        for (int k4 = 0; k4 < 4; ++k4) {
          f32x4 v = {acc[tl][4 * k4], acc[tl][4 * k4 + 1], acc[tl][4 * k4 + 2], acc[tl][4 * k4 + 3]};
          *(LDSP f32x4*)(sb + ((tl * 3 + src) * 4 + k4) * KB) = v;
        }
      } else {
        r = acc[tl];
      }
    }
    BARRIER();
#pragma unroll
    for (int src = 0; src < 3; ++src)
#pragma unroll
      for (int k4 = 0; k4 < 4; ++k4) {
        const f32x4 v = *(LDSP f32x4*)(sb + ((c.w * 3 + src) * 4 + k4) * KB);
#pragma unroll
        for (int e = 0; e < 4; ++e) r[4 * k4 + e] += v[e];
      }
    float s = 0.f, ss = 0.f;
#pragma unroll
    for (int q = 0; q < 8; ++q) {
      xres[q] = pack_bf16(bf_lo(xres[q]) + r[2 * q], bf_hi(xres[q]) + r[2 * q + 1]);
      const float lo = bf_lo(xres[q]), hi = bf_hi(xres[q]);
      s += lo + hi; ss = fmaf(lo, lo, fmaf(hi, hi, ss));
    }
    const unsigned tf = c.p * 4 + 2 * th + (c.w >> 1), tile = 2 * fc + (c.w & 1);
    if (DOWN && L == 1) {
#pragma unroll
      for (int u = 0; u < 2; ++u) {
        u32x4 v = {xres[4 * u], xres[4 * u + 1], xres[4 * u + 2], xres[4 * u + 3]};
        *(u32x4*)(out + (size_t)(tf * 32 + c.tok) * D_ + tile * 32 + 16 * u + 8 * c.hf) = v;
      }
    } else {
      constexpr unsigned xo = OFF_X + (L * 2 + (DOWN ? 2 : 1)) * X_SZ;
#pragma unroll
      for (int u = 0; u < 2; ++u) {
        u32x4 v = {xres[4 * u], xres[4 * u + 1], xres[4 * u + 2], xres[4 * u + 3]};
        __builtin_amdgcn_raw_buffer_store_b128(v, c.rs, c.l16, xo + (tf * 28 + 2 * tile + u) * KB, SC1);
      }
      f32x2 st = {xor32_sum(s), xor32_sum(ss)};
      if (c.lane < 32) __builtin_amdgcn_raw_buffer_store_b64(__builtin_bit_cast(u32x2, st), c.rs, c.lane * 8u, xo + X_STAT + (tf * 14 + tile) * 256, SC1);
    }
    STAMP(SI + 2);
  } else {
    BARRIER();
  }
  if (DOWN && L == 1) return;
  VMCNT(0);
  BARRIER();
  if (c.j < 14) set_flag(c, L * 4 + (DOWN ? 3 : 1));
  if (!DOWN) {
    dma_group(c, OFF_WGU + (c.j * 7 + 3) * SLOT, L ? 0 : 3);
    dma_group(c, OFF_WGU + (c.j * 7 + 4) * SLOT, L ? 1 : 4);
  }
}

struct GuEpi { f32x4 cs[4]; float h[8]; float mean, rstd, gt, up, t; };
template <int L, int n>
DI void gu_part(const Ctx& c, GuEpi& e, LDSP char* csb, f32x16 (&acc)[7]) {
  constexpr int g = n >> 5, i = (n >> 2) & 7, ph = n & 3;
  if (ph == 0) {
    if (i == 0) PIN(acc[g]);
    e.gt = (acc[g][i] - e.mean * e.cs[i >> 2][i & 3]) * e.rstd; e.t = e.gt * -1.4426950408889634f; PIN(e.t);
  } else if (ph == 1) {
    e.t = __builtin_amdgcn_exp2f(e.t); e.up = (acc[g][8 + i] - e.mean * e.cs[2 + (i >> 2)][i & 3]) * e.rstd; PIN(e.t); PIN(e.up);
    if ((i & 3) == 3 && g < 6) {
      e.cs[i >> 2] = *(LDSP f32x4*)(csb + ((4 + g) * 32 + (i >> 2) * 4) * 4);
      e.cs[2 + (i >> 2)] = *(LDSP f32x4*)(csb + ((4 + g) * 32 + 8 + (i >> 2) * 4) * 4);
    }
  } else if (ph == 2) {
    e.t = __builtin_amdgcn_rcpf(1.0f + e.t); e.up *= e.gt; PIN(e.t); PIN(e.up);
  } else {
    e.h[i] = e.t * e.up; PIN(e.h[i]);
    if (i == 7) {
      u32x4 pk;
#pragma unroll
      for (int q = 0; q < 4; ++q) pk[q] = pack_bf16(e.h[2 * q], e.h[2 * q + 1]);
      __builtin_amdgcn_raw_buffer_store_b128(pk, c.rs, c.l16, OFF_H + L * H_SZ + ((c.p * 4 + c.w) * 112 + c.j * 7 + g) * KB, SC1);
    }
  }
}
template <int L>
DI void stage_gu(Ctx& c, bf16x8 (&Wd)[56]) {
  constexpr unsigned SB = L ? 2 : 0;
  const int jj = min(c.j, 13), th = jj / 7, fc = jj - th * 7;
  wait_flags(c, ((L * 4 + 1) * 16 + c.p) * 64 + 7 * (c.w >> 1) + c.lane % 7);
  STAMP(L * 16 + 8);
  bf16x8 B[28]; f32x2 st[7];
  load_tokens<SC1>(c, OFF_X + (L * 2 + 1) * X_SZ, B, st);
  LDSP char* const b01 = lds_base(c, c.l16), * const b23 = lds_base(c, 2 * SLOT + c.l16), * const b4 = lds_base(c, 4 * SLOT + c.l16);
  LDSP char* const sl[5] = {b01, b01 + SLOT, b23, b23 + SLOT, b4};
  LDSP char* const wb[7] = {sl[SB % 5], sl[(SB + 1) % 5], sl[(SB + 2) % 5], sl[(SB + 3) % 5], sl[(SB + 4) % 5], sl[(SB + 5) % 5], sl[(SB + 6) % 5]};
  LDSP char* const csb = lds_base(c, LDS_CS + c.hf * 64);
  f32x16 acc[7];
  GuEpi e;
  gemm_stream<7, 4>(wb, B, acc, [&](auto S) __attribute__((always_inline)) {
    constexpr int s = S;
    if constexpr (s == 4) token_stats(st, e.mean, e.rstd);
    if constexpr (s == 8) {
#pragma unroll
      for (int k4 = 0; k4 < 4; ++k4) e.cs[k4] = *(LDSP f32x4*)(csb + (3 * 32 + k4 * 4) * 4);
    }
    if constexpr (s == 40) RAW_BARRIER();
    if constexpr (s == 56) RAW_BARRIER();
    if constexpr (s >= 57 && s < 71) dma_frag(c, OFF_WGU + (c.j * 7 + 5 + (s - 57) / 7) * SLOT, (SB + 5 + (s - 57) / 7) % 5, (s - 57) % 7);
    if constexpr (L == 0 && s >= 72 && s < 128 && (s & 1) == 0) { const int i = 28 + (s - 72) / 2; ld_frag_agpr(c, Wd[i], OFF_WD + ((fc * 2 + (i & 1)) * 112 + c.w * 28 + (i >> 1)) * KB); }
    if constexpr (s == 134) { if (L == 0) VMCNT(30); else VMCNT(2); RAW_BARRIER(); }
    if constexpr (s == 141) RAW_BARRIER();
    if constexpr (s >= 60 && s < 116) {
      static_for<(s - 60) * 64 / 56, (s - 59) * 64 / 56>([&](auto N) __attribute__((always_inline)) { gu_part<L, N>(c, e, csb, acc); });
    }
    if constexpr (s >= 116) {
      static_for<64 + (s - 116) * 128 / 80, 64 + (s - 115) * 128 / 80>([&](auto N) __attribute__((always_inline)) { gu_part<L, N>(c, e, csb, acc); });
    }
  });
  STAMP(L * 16 + 10);
  mfma_done(acc[6]);
  {
    u32x4 pk; float h[8];
#pragma unroll
    for (int i = 0; i < 8; ++i) {
      const float gt = (acc[6][i] - e.mean * e.cs[i >> 2][i & 3]) * e.rstd, up = (acc[6][8 + i] - e.mean * e.cs[2 + (i >> 2)][i & 3]) * e.rstd;
      h[i] = gt * up * __builtin_amdgcn_rcpf(1.0f + __builtin_amdgcn_exp2f(gt * -1.4426950408889634f));
    }
#pragma unroll
    for (int q = 0; q < 4; ++q) pk[q] = pack_bf16(h[2 * q], h[2 * q + 1]);
    __builtin_amdgcn_raw_buffer_store_b128(pk, c.rs, c.l16, OFF_H + L * H_SZ + ((c.p * 4 + c.w) * 112 + c.j * 7 + 6) * KB, SC1);
  }
  FENCE();
  STAMP(L * 16 + 11);
  if (L == 0) {
#pragma unroll
    for (int g = 0; g < 3; ++g) dma_group(c, OFF_WQKV + (c.j * 3 + g) * SLOT, 2 + g);
    VMCNT(21);
  } else {
    VMCNT(0);
  }
  BARRIER();
  set_flag(c, L * 4 + 2);
}

__global__ void __launch_bounds__(256, 1) main_kernel(char* ws, __hip_bfloat16* out) {
  extern __shared__ __attribute__((aligned(16))) char smem[];
  Ctx c;
  c.rs = __builtin_amdgcn_make_buffer_rsrc(ws, 0, WS_USED, 0x00020000);
  c.tid = threadIdx.x; c.lane = threadIdx.x & 63; c.l16 = c.lane * 16u; c.tok = c.lane & 31; c.hf = c.lane >> 5;
  c.w = __builtin_amdgcn_readfirstlane(threadIdx.x >> 6);
  const int r = blockIdx.x >> 3;
  c.p = 2 * (blockIdx.x & 7) + (r >> 4); c.j = r & 15;
  c.lds = (LDSP char*)smem;
  STAMP(0);
#pragma unroll
  for (int g = 0; g < 3; ++g) dma_group(c, OFF_WQKV + (c.j * 3 + g) * SLOT, g);
  if (c.w < 2) {
    unsigned keep;
    asm volatile("s_mov_b32 %0, m0\n\ts_mov_b32 m0, %3\n\ts_nop 4\n\tbuffer_load_dwordx4 %1, %2, %4 offen lds\n\ts_mov_b32 m0, %0"
                 : "=&s"(keep) : "v"(c.l16), "s"(c.rs), "s"(LDS_CS + c.w * KB), "s"(OFF_CS + c.j * 1280u + c.w * KB) : "memory");
  }
  bf16x8 Wo[16], Wd[56];
  unsigned xres[8];
  stage_qkv<0>(c, Wo, Wd, xres);
  relaunder(c);
  stage_proj<0, false, 16>(c, Wo, xres, out);
  relaunder(c);
  stage_gu<0>(c, Wd);
  relaunder(c);
  stage_proj<0, true, 56>(c, Wd, xres, out);
  relaunder(c);
  stage_qkv<1>(c, Wo, Wd, xres);
  relaunder(c);
  stage_proj<1, false, 16>(c, Wo, xres, out);
  relaunder(c);
  stage_gu<1>(c, Wd);
  relaunder(c);
  stage_proj<1, true, 56>(c, Wd, xres, out);
}

extern "C" void kernel_launch(void* const* d_in, const int* in_sizes, int n_in, void* d_out, int out_size, void* d_ws,
                              size_t ws_size, hipStream_t stream) {
  const __hip_bfloat16* x = (const __hip_bfloat16*)d_in[0];
  const __hip_bfloat16* wqkv = (const __hip_bfloat16*)d_in[1];
  const __hip_bfloat16* wo = (const __hip_bfloat16*)d_in[2];
  const __hip_bfloat16* wg = (const __hip_bfloat16*)d_in[4];
  const __hip_bfloat16* wu = (const __hip_bfloat16*)d_in[5];
  const __hip_bfloat16* wd = (const __hip_bfloat16*)d_in[6];
  (void)hipFuncSetAttribute((const void*)main_kernel, hipFuncAttributeMaxDynamicSharedMemorySize, LDS_BYTES);
  prep_kernel<<<282, 256, 0, stream>>>(x, wqkv, wo, wg, wu, wd, (char*)d_ws);
  main_kernel<<<256, 256, LDS_BYTES, stream>>>((char*)d_ws, (__hip_bfloat16*)d_out);
}
```

```cpp
#include <hip/hip_runtime.h>
#include <hip/hip_bf16.h>
#include <utility>

typedef __attribute__((ext_vector_type(8))) __bf16 bf16x8;
typedef __attribute__((ext_vector_type(2))) __bf16 bf16x2;
typedef __attribute__((ext_vector_type(16))) float f32x16;
typedef __attribute__((ext_vector_type(4))) float f32x4;
typedef __attribute__((ext_vector_type(2))) float f32x2;
typedef __attribute__((ext_vector_type(4))) unsigned u32x4;
typedef __attribute__((ext_vector_type(2))) unsigned u32x2;
typedef __amdgpu_buffer_rsrc_t rsrc_t;
#define DI __device__ __forceinline__
#define LDSP __attribute__((address_space(3)))

constexpr int S_ = 2048, D_ = 448, F_ = 1792;
constexpr unsigned KB = 1024;
constexpr unsigned OFF_FLAG = 0;
constexpr unsigned OFF_EPOCH = 28 * KB;
constexpr unsigned GRP      = 29 * KB;
constexpr unsigned OFF_WQKV = 40 * KB;
constexpr unsigned OFF_WO   = OFF_WQKV + 48 * GRP;
constexpr unsigned OFF_WGU  = OFF_WO + 448 * KB;
constexpr unsigned OFF_WD   = OFF_WGU + 112 * GRP;
constexpr unsigned OFF_FLAG0 = OFF_WD + 1568 * KB;
constexpr unsigned OFF_DBG  = OFF_FLAG0 + 256 * KB;
constexpr unsigned PAN      = 1568 * KB;
constexpr unsigned X_SZ     = 128 * KB;
constexpr unsigned X_STAT   = 112 * KB;
constexpr unsigned OFF_X    = 5 * PAN - X_SZ;
constexpr unsigned OFF_O    = OFF_X + 4 * X_SZ;
constexpr unsigned O_SZ     = 128 * KB;
constexpr unsigned OFF_H    = OFF_O + 2 * O_SZ;
constexpr unsigned H_SZ     = 448 * KB;
constexpr unsigned OFF_FLAGS = OFF_H + 2 * H_SZ;
constexpr unsigned WS_USED  = 21 * PAN;
constexpr unsigned SLOT = GRP;
constexpr unsigned LDS_BYTES = 154 * KB;
constexpr unsigned KV_HALO = 2 * 4 * 2 * 144 * 16;
constexpr int SC1 = 16;

DI unsigned pack_bf16(float a, float b) {
  bf16x2 v; v[0] = (__bf16)a; v[1] = (__bf16)b;
  return __builtin_bit_cast(unsigned, v);
}
DI float bf_lo(unsigned u) { return __builtin_bit_cast(float, u << 16); }
DI float bf_hi(unsigned u) { return __builtin_bit_cast(float, u & 0xffff0000u); }
DI float xor32_sum(float s) {
  unsigned a = __builtin_bit_cast(unsigned, s);
  auto r = __builtin_amdgcn_permlane32_swap(a, a, false, false);
  return __builtin_bit_cast(float, (unsigned)r[0]) + __builtin_bit_cast(float, (unsigned)r[1]);
}

DI int frag_row(int i, int hf) { return (i & 3) + 8 * (i >> 2) + 4 * hf; }

#define BARRIER() asm volatile("s_waitcnt lgkmcnt(0)\n\ts_barrier" ::: "memory")
#define RAW_BARRIER() asm volatile("s_barrier" ::: "memory")
#define VMCNT(n) asm volatile("s_waitcnt vmcnt(" #n ")" ::: "memory")
#define FENCE() asm volatile("" ::: "memory")
#define SCHED() __builtin_amdgcn_sched_barrier(0)
#define PIN(x) asm volatile("" : "+v"(x))
#define STAMP(i) do {} while (0)
#define CSTAMP(i) do {} while (0)

struct Ctx {
  rsrc_t rs; unsigned l16, tid, tag, early; int lane, w, p, j, tok, hf;
  LDSP char* lds;
};

template <int AUX>
DI bf16x8 ld_frag(const Ctx& c, unsigned off) {
  return __builtin_bit_cast(bf16x8, __builtin_amdgcn_raw_buffer_load_b128(c.rs, c.l16, off, AUX));
}
DI void ld_frag_agpr(const Ctx& c, bf16x8& dst, unsigned off) {
  asm volatile("buffer_load_dwordx4 %0, %1, %2, %3 offen sc1" : "=a"(dst) : "v"(c.l16), "s"(c.rs), "s"(off) : "memory");
}
DI void st_frag(const Ctx& c, const u32x4& v, unsigned off) {
  asm volatile("buffer_store_dwordx4 %0, %1, %2, %3 offen sc1\n\ts_nop 1" :: "v"(v), "v"(c.l16), "s"(c.rs), "s"(off) : "memory");
}
DI void relaunder(Ctx& c) {
  asm volatile("" : "+v"(c.tid));
  asm volatile("" : "+s"(c.w), "+s"(c.p), "+s"(c.j));
  c.lane = c.tid & 63; c.l16 = c.lane * 16u; c.tok = c.lane & 31; c.hf = c.lane >> 5;
}
DI LDSP char* lds_base(const Ctx& c, unsigned off) {
  unsigned v = off; PIN(v);
  return c.lds + v;
}
template <bool FIRST> DI void mfma_v(f32x16& acc, const bf16x8& a, const bf16x8& b) {
  if (FIRST) asm volatile("v_mfma_f32_32x32x16_bf16 %0, %1, %2, 0" : "=&v"(acc) : "v"(a), "v"(b));
  else asm volatile("v_mfma_f32_32x32x16_bf16 %0, %1, %2, %0" : "+v"(acc) : "v"(a), "v"(b));
}
template <bool FIRST> DI void mfma_a(f32x16& acc, const bf16x8& a, const bf16x8& b) {
  if (FIRST) asm volatile("v_mfma_f32_32x32x16_bf16 %0, %1, %2, 0" : "=&v"(acc) : "a"(a), "v"(b));
  else asm volatile("v_mfma_f32_32x32x16_bf16 %0, %1, %2, %0" : "+v"(acc) : "a"(a), "v"(b));
}
DI void mfma_done(f32x16& acc) { asm volatile("s_nop 13" : "+v"(acc)); }

template <int I>
DI void dma_piece(const Ctx& c, unsigned src, unsigned slot) {
  constexpr int I0 = I & 4;
  if (I == 7)
    asm volatile("s_mov_b32 m0, %2\n\ts_nop 0\n\tbuffer_load_dword %0, %1, %3 offen sc1 lds"
                 :: "v"((c.lane & 3) * 128u + (c.lane >> 2) * 4u), "s"(c.rs), "s"(slot * SLOT + 28 * KB + c.w * 256u),
                    "s"(src + 28 * KB + (c.w >> 1) * 512u + (c.w & 1) * 64u) : "memory");
  else if (I == I0)
    asm volatile("s_mov_b32 m0, %2\n\ts_nop 0\n\tbuffer_load_dwordx4 %0, %1, %3 offen sc1 lds"
                 :: "v"(c.l16), "s"(c.rs), "s"(slot * SLOT + (c.w * 7 + I0) * KB), "s"(src + (c.w * 7 + I0) * KB) : "memory");
  else
    asm volatile("buffer_load_dwordx4 %0, %1, %2 offen offset:%3 sc1 lds"
                 :: "v"(c.l16), "s"(c.rs), "s"(src + (c.w * 7 + I0) * KB), "i"((I - I0) * 1024) : "memory");
}
template <int B0, class F, int... I>
DI void static_for_impl(F&& f, std::integer_sequence<int, I...>) { (f(std::integral_constant<int, B0 + I>{}), ...); }
template <int B0, int E0, class F>
DI void static_for(F&& f) { static_for_impl<B0>(f, std::make_integer_sequence<int, (E0 > B0 ? E0 - B0 : 0)>{}); }
#define LAMBDA(X) [&](auto X) __attribute__((always_inline))
DI void dma_group(const Ctx& c, unsigned src, unsigned slot) { static_for<0, 8>(LAMBDA(I) { dma_piece<I>(c, src, slot); }); }
constexpr int down_ks(int k) { return k < 12 ? (k / 3) * 7 + k % 3 : ((k - 12) >> 2) * 7 + 3 + ((k - 12) & 3); }
template <int I0, int I1>
DI void load_wd(const Ctx& c, bf16x8 (&Wd)[56]) {
  const int fc = min(c.j & 7, 6);
  static_for<I0, I1>(LAMBDA(I) { ld_frag_agpr(c, Wd[I], OFF_WD + ((fc * 2 + ((I ^ c.w) & 1)) * 112 + c.w * 28 + down_ks(I >> 1)) * KB); });
}

DI void wait_flags(const Ctx& c, int word) {
#pragma unroll 1
  for (int spin = 0; spin < (1 << 16); ++spin) {
    const unsigned v = __builtin_amdgcn_raw_buffer_load_b32(c.rs, (unsigned)word * 4u, OFF_FLAG, SC1);
    if (__all(v == c.tag)) break;
    FENCE();
  }
  FENCE();
}
DI int flag_word(int stage, int p, int j) { return (OFF_FLAGS / 128 + p * (PAN / 128) + stage * 16 + j) * 32; }
DI void set_flag(const Ctx& c, int stage) {
  if (c.tid == 0) __builtin_amdgcn_raw_buffer_store_b32(c.tag, c.rs, (unsigned)flag_word(stage, c.p, c.j) * 4u, OFF_FLAG, SC1);
  FENCE();
}
DI int x_src(const Ctx& c, int tf) { return 8 * (tf >> 1) + min((c.lane >> 2) & 7, 6); }
DI float finish_stats(float s, float ss, bf16x8& mfrag, bool live) {
  const float mean = s * (1.0f / D_);
  const unsigned m = live ? pack_bf16(-mean, -mean) : 0u;
  mfrag = __builtin_bit_cast(bf16x8, u32x4{m, m, m, m});
  PIN(mfrag);
  return fmaxf(ss * (1.0f / D_) - mean * mean, 0.f) + 1e-5f;
}
template <int AUX>
DI f32x2 ld_stat(const Ctx& c, unsigned voff, unsigned soff) {
  return __builtin_bit_cast(f32x2, __builtin_amdgcn_raw_buffer_load_b64(c.rs, voff, soff, AUX));
}
DI float token_stats(const f32x2 (&st)[7], bf16x8& mfrag) {
  const float s = ((st[0][0] + st[1][0]) + (st[2][0] + st[3][0])) + ((st[4][0] + st[5][0]) + st[6][0]);
  const float ss = ((st[0][1] + st[1][1]) + (st[2][1] + st[3][1])) + ((st[4][1] + st[5][1]) + st[6][1]);
  return finish_stats(xor32_sum(s), xor32_sum(ss), mfrag, true);
}

template <int NP> constexpr int st_grp_(int s) { return s < 29 * NP ? s % NP : s / 29; }
template <int NP> constexpr int st_ks_(int s) { return s < 29 * NP ? s / NP : s % 29; }
#define st_grp st_grp_<NP>
#define st_ks st_ks_<NP>
template <int NG, int NP, bool PRE, class Fill>
DI void gemm_stream(const Ctx& c, LDSP char* const (&base)[NG], unsigned xoff, bf16x8 (&B)[29], f32x16 (&acc)[NG], float& var, Fill fill) {
  constexpr int PD = 6, NS = NG * 29, NB = PRE ? 28 : 12, AUX = SC1;
  const unsigned tf = c.w;
  xoff += c.p * PAN;
  bf16x8 a[NS]; f32x2 st[7];
  if constexpr (!PRE) static_for<0, NB>(LAMBDA(K) { B[K] = ld_frag<AUX>(c, xoff + (tf * 28 + K) * KB); });
  static_for<0, PD>(LAMBDA(S) { a[S] = *(LDSP bf16x8*)(base[st_grp(S)] + st_ks(S) * KB); });
  static_for<0, NS>(LAMBDA(S) {
    constexpr int s = S;
    SCHED();
    mfma_v<st_ks(s) == 0>(acc[st_grp(s)], a[s], B[st_ks(s)]);
    SCHED();
    if constexpr (s + PD < NS) a[s + PD] = *(LDSP bf16x8*)(base[st_grp(s + PD)] + st_ks(s + PD) * KB);
    if constexpr ((s & 1) == 0 && s / 2 + NB < 28) B[s / 2 + NB] = ld_frag<AUX>(c, xoff + (tf * 28 + s / 2 + NB) * KB);
    if constexpr (!PRE && s >= 32 && s < 39) st[s - 32] = ld_stat<AUX>(c, c.lane * 8u, xoff + X_STAT + (tf * 14 + 2 * (s - 32)) * 256);
    if constexpr (!PRE && s == 28 * NP - 2 * (NP - 1) * (NP - 1)) { var = token_stats(st, B[28]); PIN(var); }
    fill(S);
  });
  SCHED();
}

struct Inputs { const __hip_bfloat16 *x, *wqkv, *wo, *wg, *wu, *wd; };
DI void sum_frag(bf16x8 f, f32x4& s, f32x4& ss) {
  const u32x4 v = __builtin_bit_cast(u32x4, f);
#pragma unroll
  for (int k = 0; k < 4; k += 2) {
    const f32x4 t = {bf_lo(v[k]), bf_hi(v[k]), bf_lo(v[k + 1]), bf_hi(v[k + 1])};
    s += t; ss = __builtin_elementwise_fma(t, t, ss);
  }
}
DI void dma_plain(rsrc_t rs, unsigned voff, unsigned lds, unsigned soff) {
  asm volatile("s_mov_b32 m0, %2\n\ts_nop 0\n\tbuffer_load_dwordx4 %0, %1, %3 offen lds" :: "v"(voff), "s"(rs), "s"(lds), "s"(soff) : "memory");
}
DI int flag0_word(int wg) { return (OFF_FLAG0 + wg * KB + (blockIdx.x & 7) * 128) / 4; }
DI void set_flag0(const Ctx& c) {
  if (c.tid < 8) __builtin_amdgcn_raw_buffer_store_b32(c.tag, c.rs, c.tid * 128u, OFF_FLAG0 + blockIdx.x * KB, SC1);
  FENCE();
}
DI void phase0(Ctx& c, const Inputs& in, bf16x8 (&B)[29], float& var, unsigned (&xres)[8]) {
  constexpr int T_QKV = 42, T_GU = 112, T_WD = 56, T_WO = 16;
  const unsigned epoch = __builtin_amdgcn_raw_buffer_load_b32(c.rs, 0u, OFF_EPOCH, SC1);
  unsigned long long pc; unsigned code_bytes;
  asm volatile("s_getpc_b64 %0\n.Lcode_begin:\n\ts_mov_b32 %1, .Lcode_end-.Lcode_begin" : "=s"(pc), "=s"(code_bytes));
  const unsigned coff = min((((blockIdx.x >> 3) * 4u + c.w) & 15u) * 4096u + c.lane * 64u, code_bytes - 4u);
  const unsigned warm = *(const __attribute__((address_space(1))) unsigned*)(pc + coff);
  FENCE();
  auto tag_of = [&](unsigned e) __attribute__((always_inline)) { PIN(e); asm volatile("" :: "v"(warm)); return __builtin_amdgcn_readfirstlane(e) + 0x9E3779B9u; };
  const int t = c.tid, tc = min(t, 223);
  int b = blockIdx.x;
  const int kind = b < T_QKV ? 0 : (b < T_QKV + T_GU ? 1 : (b < T_QKV + T_GU + T_WD ? 2 : (b < T_QKV + T_GU + T_WD + T_WO ? 3 : 4)));
  b -= kind == 0 ? 0 : (kind == 1 ? T_QKV : (kind == 2 ? T_QKV + T_GU : (kind == 3 ? T_QKV + T_GU + T_WD : b)));
  const int cb = b / 7, chk = b - cb * 7;
  const int rg = kind < 2 ? tc / 28 : tc / 56, cc = tc - rg * (kind < 2 ? 28 : 56), half = cc / 14, c14 = cc - half * 14;
  const __hip_bfloat16* src; int ld = D_, nv = 8;
  if (kind == 0) { ld = 3 * D_; src = in.wqkv + (chk * 64 + rg * 8) * ld + cb * 224 + cc * 8; }
  else if (kind == 1) { ld = F_; src = (half ? in.wu : in.wg) + (chk * 64 + rg * 8) * ld + cb * 112 + c14 * 8; }
  else if (kind == 3) { src = in.wo + (b * 28 + 14 * (rg & 1) + 8 * (rg >> 1)) * D_ + cc * 8; nv = rg >> 1 ? 6 : 8; }
  else src = in.wd + (b * 32 + rg * 8) * D_ + cc * 8;
  u32x4 wa[8], wb[8], hx[4];
  STAMP(32);
  auto load_x = [&](int r0, int r1) __attribute__((always_inline)) {
    const rsrc_t xs = __builtin_amdgcn_make_buffer_rsrc((void*)in.x, 0, S_ * D_ * 2, 0x00020000);
    if (c.lane < 56) {
#pragma unroll
      for (int i = r0; i < r1; ++i) {
        const unsigned row = c.w * 32 + ((i + 4 * c.j) & 31);
        dma_plain(xs, c.l16, row * 912u, (c.p * 128 + row) * 896u);
      }
    }
#pragma unroll
    for (int q = 0; q < (r1 == 32 ? 4 : 0); ++q) {
      const unsigned e = min(c.tid + 256u * q, 895u);
      hx[q] = __builtin_amdgcn_raw_buffer_load_b128(xs, (e & 15) * 896u + (e >> 4) * 16u, __builtin_amdgcn_readfirstlane(max(c.p * 128 - 16, 0) * 896), 0);
    }
    FENCE();
  };
  auto load_w = [&](u32x4 (&w8)[8]) __attribute__((always_inline)) {
#pragma unroll
    for (int r = 0; r < 8; ++r) w8[r] = *(const u32x4*)(src + min(r, nv - 1) * ld);
    FENCE();
  };
  if (kind == 0) load_w(wa); else load_x(0, 32);
  load_w(wb);
  LDSP u32x4* const img = (LDSP u32x4*)lds_base(c, 114 * KB);
  LDSP float* const red = (LDSP float*)(img + 32 * 64);
  auto group_off = [&](int fr) -> unsigned {
    if (kind == 0) { const int hb = (cb * 2 + (fr >> 2)) * 4 + (fr & 3); return OFF_WQKV + ((hb & 15) * 3 + (hb >> 4)) * GRP; }
    return OFF_WGU + (cb * 7 + fr) * GRP;
  };
  int idx[8], csi; unsigned cso;
  {
    const int ib = kind == 1 ? ((c14 >> 1) * 4 + (rg >> 1)) * 64 + 32 * (rg & 1) + 16 * half + 4 * (c14 & 1)
                             : ((cc >> 2) * 2 + (rg >> 1)) * 64 + 32 * (rg & 1) + 16 * ((cc >> 1) & 1) + 4 * (cc & 1);
    const int sw = kind == 1 ? (c14 >> 1) & 3 : (cc >> 1) & 3;
    if (kind == 0) {
      const int q14 = (c14 * 8) / 14, i0 = c14 * 8 - q14 * 14, rb = (half * 16 + (rg >> 1)) * 64 + 32 * (rg & 1);
#pragma unroll
      for (int k = 0; k < 8; ++k) {
        const int wrap = i0 + k >= 14, q = q14 + wrap, i = i0 + k - 14 * wrap;
        idx[k] = rb + (q >> 1) * 256 + (frag_row(i, q & 1) ^ (q >> 1));
      }
    } else {
#pragma unroll
      for (int k = 0; k < 8; ++k) idx[k] = ib + ((k & 3) ^ sw) + 8 * (k >> 2);
    }
#pragma unroll
    for (int k = 0; k < 8; ++k) PIN(idx[k]);
    const int fr = t >> 5, i = (t & 3) + 4 * ((t >> 3) & 3), hf = (t >> 2) & 1;
    csi = kind == 0 ? (i < 14 ? fr * 28 + 14 * hf + i : -1) : min((i >> 3) * 112 + fr * 16 + hf * 8 + (i & 7), 223);
    cso = group_off(fr) + 28 * KB + chk * 128 + (t & 31) * 4;
    PIN(cso);
  }
  if (kind == 0) img[(t >> 3) * 64 + 32 * ((t >> 2) & 1) + (frag_row(14 + (t & 1), (t >> 1) & 1) ^ ((t >> 5) & 3))] = u32x4{0u, 0u, 0u, 0u};
  auto transpose = [&](u32x4 (&w8)[8]) __attribute__((always_inline)) {
  if (nv < 8) { w8[6] = u32x4{0u, 0u, 0u, 0u}; w8[7] = w8[6]; }
  if (t < 224) {
#pragma unroll
    for (int k = 0; k < 8; ++k) {
      u32x4 o;
#pragma unroll
      for (int q = 0; q < 4; ++q) o[q] = __builtin_amdgcn_perm(w8[2 * q + 1][k >> 1], w8[2 * q][k >> 1], (k & 1) ? 0x07060302u : 0x05040100u);
      img[idx[k]] = o;
    }
    if (kind < 2) {
      f32x4 cs[2];
#pragma unroll
      for (int k2 = 0; k2 < 4; ++k2) {
        f32x2 sm = {bf_lo(w8[0][k2]), bf_hi(w8[0][k2])};
#pragma unroll
        for (int r = 1; r < 8; ++r) sm += f32x2{bf_lo(w8[r][k2]), bf_hi(w8[r][k2])};
        cs[k2 >> 1][(k2 & 1) * 2] = sm[0]; cs[k2 >> 1][(k2 & 1) * 2 + 1] = sm[1];
      }
      *(LDSP f32x4*)(red + rg * 224 + cc * 8) = cs[0]; *(LDSP f32x4*)(red + rg * 224 + cc * 8 + 4) = cs[1];
    }
  }
  };
  u32x4 o[8]; unsigned val;
  auto fetch = [&]() __attribute__((always_inline)) {
#pragma unroll
    for (int g = 0; g < 8; ++g) o[g] = img[(g * 4 + c.w) * 64 + (c.lane ^ (kind < 2 ? g & 3 : (c.w & 2) + ((c.lane >> 4) & 1)))];
    float sm = 0.f;
#pragma unroll
    for (int r = 0; r < 8; ++r) sm += red[r * 224 + max(csi, 0)];
    const float hi = bf_lo(pack_bf16(sm, 0.f));
    val = csi >= 0 ? pack_bf16(hi, sm - hi) : 0u;
  };
  auto store = [&]() __attribute__((always_inline)) {
    if (kind < 4) {
#pragma unroll
      for (int g = 0; g < 8; ++g) {
        const int f = g * 4 + c.w;
        if (g < 7 || kind == 0)
          st_frag(c, o[g], kind < 2 ? group_off(g) + (chk * 4 + c.w) * KB
                                    : (kind == 3 ? OFF_WO : OFF_WD) + ((f >> 1) * (kind == 3 ? 32 : 112) + 2 * b + (f & 1)) * KB);
      }
    }
    if (kind < 2 && (t < 224 || kind == 0)) {
      __builtin_amdgcn_raw_buffer_store_b32(val, c.rs, cso, 0u, SC1);
      if (chk == 6) __builtin_amdgcn_raw_buffer_store_b32(0u, c.rs, cso, 128u, SC1);
    }
    FENCE();
  };
  STAMP(33);
  VMCNT(8);
  c.tag = tag_of(epoch);
  if (kind == 0) {
    transpose(wa);
    STAMP(34);
    BARRIER();
    fetch(); store(); VMCNT(0); BARRIER(); set_flag0(c);
    STAMP(35);
    load_x(0, 32);
    VMCNT(0);
  }
#pragma unroll
  for (int q = 0; q < 4; ++q) PIN(hx[q]);
  BARRIER();
  STAMP(36);
  {
    LDSP char* const xb = lds_base(c, (c.w * 32 + c.tok) * 912 + c.hf * 16);
#pragma unroll
    for (int ks = 0; ks < 28; ++ks) B[ks] = *(LDSP bf16x8*)(xb + ks * 32);
    const int th = c.j >> 3, fc = min(c.j & 7, 6);
#pragma unroll
    for (int u = 0; u < 2; ++u) {
      const u32x4 v = *(LDSP u32x4*)(c.lds + ((2 * th + (c.w >> 1)) * 32 + c.tok) * 912 + (4 * fc + 2 * (c.w & 1) + u) * 32 + c.hf * 16);
#pragma unroll
      for (int q = 0; q < 4; ++q) xres[4 * u + q] = v[q];
    }
  }
#pragma unroll
  for (int r = 0; r < 8; ++r) PIN(wb[r]);
  if (kind != 0) transpose(wb);
  STAMP(37);
  if (c.w == 0) wait_flags(c, flag0_word(((min(c.lane >> 3, 2) * 16 + c.j) >> 3) * 7 + min(c.lane & 7, 6)));
  BARRIER();
  STAMP(38);
  dma_group(c, OFF_WQKV + (c.j * 3 + 1) * GRP, 1);
  dma_group(c, OFF_WQKV + (c.j * 3 + 2) * GRP, 2);
  dma_group(c, OFF_WQKV + (c.j * 3 + 0) * GRP, 0);
  if (kind != 0) { fetch(); store(); }
  BARRIER();
  STAMP(39);
  LDSP char* const hb = lds_base(c, 3 * SLOT + KV_HALO + c.l16);
#pragma unroll
  for (int q = 0; q < 4; ++q) if (q < 3 || c.tid < 128) *(LDSP u32x4*)(hb + c.w * KB + q * 4 * KB) = hx[q];
  {
    f32x4 s = {0.f, 0.f, 0.f, 0.f}, ss = s;
#pragma unroll
    for (int ks = 0; ks < 28; ++ks) { sum_frag(B[ks], s, ss); SCHED(); }
    var = finish_stats(xor32_sum((s[0] + s[1]) + (s[2] + s[3])), xor32_sum((ss[0] + ss[1]) + (ss[2] + ss[3])), B[28], true);
    PIN(var);
  }
  STAMP(25);
  if (kind == 0 || kind == 4) VMCNT(0); else VMCNT(7);
  BARRIER();
}
DI int rest_word(const Ctx& c) {
  const int l = min(c.lane, 24);
  return flag0_word(l < 14 ? 154 + 14 * c.w + l : (l < 18 ? 210 + 4 * c.w + l - 14 : 42 + 7 * c.j + l - 18));
}
DI void advance_epoch(const Ctx& c) {
  wait_flags(c, flag0_word(c.tid));
  BARRIER();
  if (c.tid == 0) __builtin_amdgcn_raw_buffer_store_b32(c.tag, c.rs, 0u, OFF_EPOCH, SC1);
}

template <int L>
DI void stage_qkv(Ctx& c, const Inputs& in, bf16x8 (&Wo)[16], bf16x8 (&Wd)[56], unsigned (&xres)[8]) {
  constexpr int AUX = SC1;
  bf16x8 B[29], hfa[15], hfb[15];
  float var, rstd, hr;
  if (L == 0) { phase0(c, in, B, var, xres); relaunder(c); }
  constexpr unsigned SB = L ? 2 : 0, SCR = (L ? 0 : 3) * SLOT;
  constexpr unsigned xoff = OFF_X + (L ? 2 : 0) * X_SZ;
  const int fc = min(c.j & 7, 6);
  if (L) {
    wait_flags(c, (c.lane < 32 || c.p == 0) ? flag_word(3, c.p, x_src(c, c.w)) : flag_word(3, c.p - 1, x_src(c, 3)));
  }
  STAMP(L * 16 + 1);
  const unsigned ptf = c.p > 0 ? 3 : c.w, pxo = xoff + max(c.p - 1, 0) * PAN;
  LDSP char* const wb[3] = {lds_base(c, (SB + 1) * SLOT + c.l16), lds_base(c, (SB + 2) * SLOT + c.l16), lds_base(c, SB * SLOT + c.l16)};
  LDSP char* const kvw = lds_base(c, SCR + (c.hf * 144 + 16 + 32 * c.w + c.tok) * 16);
  const int kv = c.w >> 1, rh = c.w & 1, hq = (c.lane >> 4) & 1, ch = 2 * rh + c.hf;
  LDSP char* const ha = lds_base(c, (SB + 1 + kv) * SLOT + c.hf * KB + (32 * hq + 16 * rh + (c.lane & 15)) * 16);
  LDSP char* const hb = lds_base(c, SCR + KV_HALO + c.l16);
  f32x16 acc[3];
  unsigned early = 0u;
  u32x4 hx[4]; f32x2 hst[4]; f32x4 a4; f32x4 hs = {0.f, 0.f, 0.f, 0.f}, hss = hs;
  if (L == 1) {
#pragma unroll
    for (int q = 0; q < 4; ++q) {
      const unsigned e = min(c.tid + 256u * q, 895u);
      hx[q] = __builtin_amdgcn_raw_buffer_load_b128(c.rs, (e >> 4) * 512 + (16 + (e & 15)) * 16, pxo + ptf * 28 * KB, AUX);
      hst[q] = ld_stat<AUX>(c, min((c.lane >> 4) + 4u * q, 13u) * 256 + (16 + (c.lane & 15)) * 8, pxo + X_STAT + ptf * 14 * 256);
    }
  }
  gemm_stream<3, 3, L == 0>(c, wb, xoff, B, acc, var, LAMBDA(S) {
    constexpr int s = S;
    if constexpr (L == 1 && s % 14 == 0) CSTAMP(40 + s / 14);
    if constexpr (L == 1 && s >= 2 && s < 6) { if (s < 5 || c.tid < 128) *(LDSP u32x4*)(hb + c.w * KB + (s - 2) * 4 * KB) = hx[s - 2]; }
    if constexpr (L == 1 && s == 14) RAW_BARRIER();
    if constexpr (s >= 16 && s < 44 && (s & 1) == 0) { constexpr int k = (s - 16) / 2; hfa[k] = *(LDSP bf16x8*)(ha + k * 2 * KB); hfb[k] = *(LDSP bf16x8*)(hb + k * KB); }
    if constexpr (s == 44) hfa[14] = *(LDSP bf16x8*)(ha + 28 * KB - c.hf * KB);
    if constexpr (s == 21) asm volatile("v_mfma_f32_16x16x32_bf16 %0, %1, %2, 0" : "=&v"(a4) : "v"(hfa[0]), "v"(hfb[0]));
    if constexpr (s > 21 && s < 49 && (s & 1) == 1) asm volatile("v_mfma_f32_16x16x32_bf16 %0, %1, %2, %0" : "+v"(a4) : "v"(hfa[(s - 21) / 2]), "v"(hfb[(s - 21) / 2]));
    if constexpr (L == 0 && s >= 22 && s < 50 && (s & 1) == 0) { sum_frag(hfb[(s - 22) / 2], hs, hss); PIN(hs); PIN(hss); }
    if constexpr (s == 50) {
      float t = (hs[0] + hs[1]) + (hs[2] + hs[3]), tt = (hss[0] + hss[1]) + (hss[2] + hss[3]);
      if (L == 1) {
        t = hst[0][0] + hst[1][0] + hst[2][0]; tt = hst[0][1] + hst[1][1] + hst[2][1];
        if (c.lane < 32) { t += hst[3][0]; tt += hst[3][1]; }
      }
      t += __shfl_xor(t, 16); tt += __shfl_xor(tt, 16);
      hr = __builtin_amdgcn_rsqf(finish_stats(xor32_sum(t), xor32_sum(tt), hfb[14], c.lane < 32));
      if (c.p == 0) hr = 0.f;
      PIN(hr);
    }
    if constexpr (s == 52) asm volatile("v_mfma_f32_16x16x32_bf16 %0, %1, %2, %0" : "+v"(a4) : "v"(hfa[14]), "v"(hfb[14]));
    if constexpr (s == 58) {
      PIN(a4);
      *(LDSP f32x4*)(c.lds + SCR + (((kv * 4 + ch) * 2 + hq) * 144 + (c.lane & 15)) * 16) = a4 * hr;
    }
    if constexpr (L == 0 && s == 6) { VMCNT(0); RAW_BARRIER(); set_flag0(c); }
    if constexpr (L == 0 && s == 40) early = __builtin_amdgcn_raw_buffer_load_b32(c.rs, rest_word(c) * 4u, OFF_FLAG, SC1);
    if constexpr (L == 0 && s == 52) { if (!__all(early == c.tag)) wait_flags(c, rest_word(c)); FENCE(); }
    if constexpr (L == 0 && s >= 54 && s < 70) load_wd<s - 54, s - 53>(c, Wd);
    if constexpr (L == 0 && s >= 70 && s < 86) {
      constexpr int i = s - 70;
      const unsigned off = OFF_WO + ((fc * 2 + (((i >> 3) ^ c.w) & 1)) * 32 + c.w * 8 + (i & 7)) * KB;
      if constexpr (i < 8) ld_frag_agpr(c, Wo[i], off); else Wo[i] = ld_frag<SC1>(c, off);
    }
    if constexpr (s == 80) { rstd = __builtin_amdgcn_rsqf(var); PIN(rstd); }
  });
  asm volatile("s_nop 11" : "+v"(acc[0]), "+v"(acc[1]), "+v"(acc[2]));
#pragma unroll
  for (int n = 0; n < 8; ++n) {
    const f32x4 v = {acc[n >> 2][4 * (n & 3)], acc[n >> 2][4 * (n & 3) + 1], acc[n >> 2][4 * (n & 3) + 2], acc[n >> 2][4 * (n & 3) + 3]};
    *(LDSP f32x4*)(kvw + n * 288 * 16) = v * rstd;
  }
  STAMP(L * 16 + 2);
  BARRIER();
  LDSP char* const rb = kvw - 14 * 16;
  const int pos = c.p * 128 + c.w * 32 + c.tok;
  float lg[8], mx = -3.0e38f, den = 0.f;
  f32x2 o[7], qv[7];
  f32x4 kvb[16][4];
  auto kv_read = [&](int s) __attribute__((always_inline)) {
#pragma unroll
    for (int k4 = 0; k4 < 3; ++k4) kvb[s][k4] = *(LDSP f32x4*)(rb + (((s >> 3) * 4 + k4) * 288 + 14 - 2 * (s & 7)) * 16);
    const f32x2 t = *(LDSP f32x2*)(rb + (((s >> 3) * 4 + 3) * 288 + 14 - 2 * (s & 7)) * 16);
    kvb[s][3][0] = t[0]; kvb[s][3][1] = t[1];
  };
  kv_read(0); kv_read(1); kv_read(2); kv_read(3);
  const float qs = rstd * (0.18898223650461361f * 1.4426950408889634f);
#pragma unroll
  for (int i = 0; i < 7; ++i) { qv[i] = f32x2{acc[2][2 * i], acc[2][2 * i + 1]} * qs; o[i] = f32x2{0.f, 0.f}; }
  static_for<0, 16>(LAMBDA(S) {
    constexpr int s = S;
    SCHED();
    if constexpr (s + 4 < 16) kv_read(s + 4);
    if constexpr (L == 0 && s < 12) load_wd<16 + s, 17 + s>(c, Wd);
    if constexpr (L == 0 && s < 10) load_wd<36 + s, 37 + s>(c, Wd);
    auto pr = [&](int i) __attribute__((always_inline)) { return f32x2{kvb[s][i >> 1][(i & 1) * 2], kvb[s][i >> 1][(i & 1) * 2 + 1]}; };
    if constexpr (s < 8) {
      f32x2 d0 = qv[0] * pr(0), d1 = qv[1] * pr(1);
#pragma unroll
      for (int i = 2; i < 7; ++i) { if (i & 1) d1 = __builtin_elementwise_fma(qv[i], pr(i), d1); else d0 = __builtin_elementwise_fma(qv[i], pr(i), d0); }
      d0 += d1;
      const float d = xor32_sum(d0[0] + d0[1]);
      lg[s] = (pos - 2 * s >= 0) ? d : -1.4426950408889634e9f;
      mx = fmaxf(mx, lg[s]);
      if constexpr (s == 7) {
#pragma unroll
        for (int k = 0; k < 8; ++k) { lg[k] = __builtin_amdgcn_exp2f(lg[k] - mx); den += lg[k]; }
      }
    } else {
#pragma unroll
      for (int i = 0; i < 7; ++i) o[i] = __builtin_elementwise_fma(f32x2{lg[s - 8], lg[s - 8]}, pr(i), o[i]);
    }
  });
  SCHED();
  const float inv = __builtin_amdgcn_rcpf(den);
#pragma unroll
  for (int u = 0; u < 2; ++u) {
    u32x4 pk;
#pragma unroll
    for (int q = 0; q < 4; ++q) { if (4 * u + q < 7) { const f32x2 t = o[4 * u + q] * inv; pk[q] = pack_bf16(t[0], t[1]); } else pk[q] = 0u; }
    st_frag(c, pk, OFF_O + L * O_SZ + c.p * PAN + (c.w * 32 + 2 * c.j + u) * KB);
  }
  STAMP(L * 16 + 3);
  VMCNT(0);
  BARRIER();
  set_flag(c, L * 4 + 0);
#pragma unroll
  for (int g = 0; g < 2; ++g) dma_group(c, OFF_WGU + (c.j * 7 + g) * GRP, SB + g);
  STAMP(L * 16 + 4);
}

template <int L, bool DOWN, int NW>
DI void stage_proj(Ctx& c, const bf16x8 (&W)[NW], bf16x8 (&Wd)[56], unsigned (&xres)[8], __hip_bfloat16* out) {
  constexpr int KW = NW / 2, KS = 4 * KW, WIN = 8;
  constexpr unsigned SCR = (DOWN || L ? 0 : 3) * SLOT;
  constexpr unsigned aoff = DOWN ? OFF_H + L * H_SZ : OFF_O + L * O_SZ;
  constexpr int SI = L * 16 + (DOWN ? 12 : 5);
  if ((c.j & 7) < 7) {
    const int th = c.j >> 3, fc = c.j & 7;
    const int fin = flag_word(L * 4 + (DOWN ? 2 : 0), c.p, 4 * c.w + (c.lane >> 4));
    if (!DOWN) wait_flags(c, fin);
    else if (!__all(c.early == c.tag)) wait_flags(c, flag_word(7 + L, c.p, 4 * c.w + (c.lane >> 4)));
    FENCE();
    auto kof = [](int k) { return (unsigned)(DOWN ? down_ks(k) : k) * KB; };
    STAMP(SI);
    const unsigned a0 = aoff + c.p * PAN + ((2 * th + (c.w >> 1)) * KS + c.w * KW) * KB, a1 = aoff + c.p * PAN + ((2 * th + 1 - (c.w >> 1)) * KS + c.w * KW) * KB;
    constexpr int DEF = 6;
    bf16x8 B[KW][2];
    static_for<0, WIN>(LAMBDA(K) { B[K][0] = ld_frag<SC1>(c, a0 + kof(K)); B[K][1] = ld_frag<SC1>(c, a1 + kof(K)); });
    if (DOWN) { FENCE(); VMCNT(16); BARRIER(); set_flag(c, L * 4 + 2); }
    LDSP char* const sb = lds_base(c, SCR + c.w * 12 * KB + c.l16);
    LDSP char* const db[3] = {lds_base(c, SCR + ((c.w ^ 1) * 12 + 0) * KB + c.l16), lds_base(c, SCR + ((c.w ^ 2) * 12 + 4) * KB + c.l16),
                              lds_base(c, SCR + ((c.w ^ 3) * 12 + 8) * KB + c.l16)};
    f32x16 acc[4];
    auto mm = [&](auto K, auto M) __attribute__((always_inline)) {
      if constexpr (DOWN) mfma_a<K == 0>(acc[M], W[2 * K + (M & 1)], B[K][M >> 1]); else if constexpr ((M & 1) == 0) mfma_a<K == 0>(acc[M], W[K], B[K][M >> 1]); else mfma_v<K == 0>(acc[M], W[(M & 1) * KW + K], B[K][M >> 1]);
    };
    static_for<0, KW>(LAMBDA(K) {
      SCHED();
      static_for<(K < KW - DEF ? 0 : 1), 4>(LAMBDA(M) { mm(K, M); });
      SCHED();
      auto ld = [&](auto N) __attribute__((always_inline)) { B[N][0] = ld_frag<SC1>(c, a0 + kof(N)); B[N][1] = ld_frag<SC1>(c, a1 + kof(N)); };
      if constexpr (DOWN && K < 4) ld(std::integral_constant<int, K + 8>{});
      if constexpr (DOWN && L == 0 && K == 3) load_wd<46, 56>(c, Wd);
      if constexpr (DOWN && K == 3) c.early = __builtin_amdgcn_raw_buffer_load_b32(c.rs, fin * 4u, OFF_FLAG, SC1);
      if constexpr (DOWN && K == 8) { if (!__all(c.early == c.tag)) wait_flags(c, fin); FENCE(); static_for<12, 18>(ld); }
      if constexpr (DOWN && K > 8 && K + 9 < KW) ld(std::integral_constant<int, K + 9>{});
    });
    STAMP(SI + 1);
    static_for<0, DEF>(LAMBDA(D) {
      SCHED();
      mm(std::integral_constant<int, KW - DEF + D>{}, std::integral_constant<int, 0>{});
      if constexpr (D == 0) asm volatile("s_nop 9" : "+v"(acc[1]), "+v"(acc[2]), "+v"(acc[3]));
      SCHED();
      static_for<2 * D, 2 * D + 2>(LAMBDA(N) {
        constexpr int m = 1 + N / 4, k4 = N % 4;
        *(LDSP f32x4*)(db[m - 1] + k4 * KB) = f32x4{acc[m][4 * k4], acc[m][4 * k4 + 1], acc[m][4 * k4 + 2], acc[m][4 * k4 + 3]};
      });
    });
    BARRIER();
    f32x4 pv[12];
#pragma unroll
    for (int n = 0; n < 12; ++n) pv[n] = *(LDSP f32x4*)(sb + ((n % 3) * 4 + n / 3) * KB);
    mfma_done(acc[0]);
    f32x16 r;
#pragma unroll
    for (int k4 = 0; k4 < 4; ++k4) {
      const f32x4 v = (pv[3 * k4] + pv[3 * k4 + 1]) + (pv[3 * k4 + 2] + f32x4{acc[0][4 * k4], acc[0][4 * k4 + 1], acc[0][4 * k4 + 2], acc[0][4 * k4 + 3]});
#pragma unroll
      for (int e = 0; e < 4; ++e) r[4 * k4 + e] = v[e];
    }
#pragma unroll
    for (int q = 0; q < 8; ++q) xres[q] = pack_bf16(bf_lo(xres[q]) + r[2 * q], bf_hi(xres[q]) + r[2 * q + 1]);
    const unsigned tf = 2 * th + (c.w >> 1), tile = 2 * fc + (c.w & 1);
    if (DOWN && L == 1) {
#pragma unroll
      for (int u = 0; u < 2; ++u) {
        u32x4 v = {xres[4 * u], xres[4 * u + 1], xres[4 * u + 2], xres[4 * u + 3]};
        *(u32x4*)(out + (size_t)(c.p * 128 + tf * 32 + c.tok) * D_ + tile * 32 + 16 * u + 8 * c.hf) = v;
      }
    } else {
      const unsigned xo = OFF_X + (L * 2 + (DOWN ? 2 : 1)) * X_SZ + c.p * PAN;
#pragma unroll
      for (int u = 0; u < 2; ++u) {
        u32x4 v = {xres[4 * u], xres[4 * u + 1], xres[4 * u + 2], xres[4 * u + 3]};
        st_frag(c, v, xo + (tf * 28 + 2 * tile + u) * KB);
      }
      SCHED();
      f32x2 s2[2] = {{0.f, 0.f}, {0.f, 0.f}}, q2[2] = {{0.f, 0.f}, {0.f, 0.f}};
#pragma unroll
      for (int q = 0; q < 8; ++q) { const f32x2 v = {bf_lo(xres[q]), bf_hi(xres[q])}; s2[q & 1] += v; q2[q & 1] = __builtin_elementwise_fma(v, v, q2[q & 1]); }
      s2[0] += s2[1]; q2[0] += q2[1];
      f32x2 st = {xor32_sum(s2[0][0] + s2[0][1]), xor32_sum(q2[0][0] + q2[0][1])};
      if (c.lane < 32) __builtin_amdgcn_raw_buffer_store_b64(__builtin_bit_cast(u32x2, st), c.rs, c.lane * 8u, xo + X_STAT + (tf * 14 + tile) * 256, SC1);
    }
    STAMP(SI + 2);
  } else {
    if (DOWN) { VMCNT(0); BARRIER(); set_flag(c, L * 4 + 2); }
    BARRIER();
  }
  if (DOWN && L == 1) return;
  VMCNT(0);
  BARRIER();
  if ((c.j & 7) < 7) set_flag(c, L * 4 + (DOWN ? 3 : 1));
  if (!DOWN) {
    dma_group(c, OFF_WGU + (c.j * 7 + 2) * GRP, L ? 4 : 2);
    dma_group(c, OFF_WGU + (c.j * 7 + 3) * GRP, L ? 0 : 3);
  }
}

struct GuEpi { float var, nl, t, pr, pp, r; unsigned pk[4]; float h0; };
template <int L, int n>
DI void gu_part(const Ctx& c, GuEpi& e, f32x16 (&acc)[7]) {
  constexpr int el = n / 3, ph = n % 3, g = el >> 3, i = el & 7, pg = (el - 1) >> 3, pi = (el - 1) & 7;
  if constexpr (ph == 0) {
    if constexpr (el > 0) { e.r = __builtin_amdgcn_rcpf(e.t); e.pp = e.pr; PIN(e.r); }
    if constexpr (g < 6) {
      if (i == 0) PIN(acc[g]);
      e.t = acc[g][i] * e.nl; e.pr = acc[g][i] * acc[g][8 + i]; PIN(e.t); PIN(e.pr);
    }
  } else if constexpr (ph == 1) {
    if constexpr (g < 6) { e.t = __builtin_amdgcn_exp2f(e.t); PIN(e.t); }
    if constexpr (el > 0) { e.pp *= e.r; PIN(e.pp); }
  } else {
    if constexpr (g < 6) { e.t = fmaf(e.t, e.var, e.var); PIN(e.t); }
    if constexpr (el > 0) {
      if constexpr ((pi & 1) == 0) e.h0 = e.pp; else { e.pk[pi >> 1] = pack_bf16(e.h0, e.pp); PIN(e.pk[pi >> 1]); }
      if constexpr (pi == 7)
        st_frag(c, u32x4{e.pk[0], e.pk[1], e.pk[2], e.pk[3]}, OFF_H + L * H_SZ + c.p * PAN + (c.w * 112 + c.j * 7 + pg) * KB);
    }
  }
}
template <int L>
DI void stage_gu(Ctx& c, bf16x8 (&Wd)[56]) {
  constexpr unsigned SB = L ? 2 : 0;
  wait_flags(c, flag_word(L * 4 + 1, c.p, x_src(c, c.w)));
  STAMP(L * 16 + 8);
  LDSP char* const b01 = lds_base(c, c.l16), * const b23 = lds_base(c, 2 * SLOT + c.l16), * const b4 = lds_base(c, 4 * SLOT + c.l16);
  LDSP char* const sl[5] = {b01, b01 + SLOT, b23, b23 + SLOT, b4};
  LDSP char* const wb[7] = {sl[SB % 5], sl[(SB + 1) % 5], sl[(SB + 2) % 5], sl[(SB + 3) % 5], sl[(SB + 4) % 5], sl[(SB + 5) % 5], sl[(SB + 6) % 5]};
  f32x16 acc[7];
  GuEpi e;
  const unsigned wsrc = OFF_WGU + c.j * 7 * GRP, qsrc = OFF_WQKV + c.j * 3 * GRP;
  bf16x8 B[29];
  gemm_stream<7, 2, false>(c, wb, OFF_X + (L * 2 + 1) * X_SZ, B, acc, e.var, LAMBDA(S) {
    constexpr int s = S;
        if constexpr (s == 55) { e.nl = __builtin_amdgcn_rsqf(e.var) * -1.4426950408889634f; PIN(e.nl); }
    if constexpr (s == 44) RAW_BARRIER();
    if constexpr (s == 58) RAW_BARRIER();
    if constexpr (s >= 56 && s < 104 && (s & 1) == 0) dma_piece<((s - 56) / 2) & 7>(c, wsrc + (4 + (s - 56) / 16) * GRP, (SB + 4 + (s - 56) / 16) % 5);
    if constexpr (s == 108) { VMCNT(17); RAW_BARRIER(); }
    if constexpr (L == 0 && s >= 110 && s < 126 && (s & 1) == 0) dma_piece<(s - 110) / 2>(c, qsrc, 2);
    if constexpr (s == 137) { if (L == 0) VMCNT(10); else VMCNT(2); RAW_BARRIER(); }
    if constexpr (L == 0 && s >= 138 && s < 146) dma_piece<s - 138>(c, qsrc + GRP, 3);
    if constexpr (L == 0 && s == 147) RAW_BARRIER();
    if constexpr (L == 0 && s >= 148 && s < 164 && (s & 1) == 0) dma_piece<(s - 148) / 2>(c, qsrc + 2 * GRP, 4);
    if constexpr (L == 0 && s >= 164 && s < 180 && (s & 1) == 0) load_wd<28 + (s - 164) / 2, 29 + (s - 164) / 2>(c, Wd);
    if constexpr (s >= 61) gu_part<L, s - 61>(c, e, acc);
    if constexpr (s == 186) c.early = __builtin_amdgcn_raw_buffer_load_b32(c.rs, flag_word(7 + L, c.p, 4 * c.w + (c.lane >> 4)) * 4u, OFF_FLAG, SC1);
    if constexpr (s == 202) PIN(c.early);
    if constexpr (s == 160) { if (L == 0) VMCNT(16); else VMCNT(1); RAW_BARRIER(); set_flag(c, 7 + L); }
  });
  STAMP(L * 16 + 10);
  static_for<203 - 61, 147>(LAMBDA(N) { gu_part<L, N>(c, e, acc); });
  mfma_done(acc[6]);
  {
    u32x4 pk; float h[8];
#pragma unroll
    for (int i = 0; i < 8; ++i)
      h[i] = acc[6][i] * acc[6][8 + i] * __builtin_amdgcn_rcpf(fmaf(__builtin_amdgcn_exp2f(acc[6][i] * e.nl), e.var, e.var));
#pragma unroll
    for (int q = 0; q < 4; ++q) pk[q] = pack_bf16(h[2 * q], h[2 * q + 1]);
    st_frag(c, pk, OFF_H + L * H_SZ + c.p * PAN + (c.w * 112 + c.j * 7 + 6) * KB);
  }
  STAMP(L * 16 + 11);
}

__global__ void __launch_bounds__(256, 1) main_kernel(Inputs in, char* ws, __hip_bfloat16* out) {
  extern __shared__ __attribute__((aligned(16))) char smem[];
  Ctx c;
  c.rs = __builtin_amdgcn_make_buffer_rsrc(ws, 0, WS_USED, 0x00020000);
  c.tid = threadIdx.x; c.lane = threadIdx.x & 63; c.l16 = c.lane * 16u; c.tok = c.lane & 31; c.hf = c.lane >> 5;
  c.w = __builtin_amdgcn_readfirstlane(threadIdx.x >> 6);
  const int r = blockIdx.x >> 3;
  c.p = 4 * ((blockIdx.x & 7) >> 1) + (r >> 3); c.j = 8 * (blockIdx.x & 1) + (r & 7);
  c.lds = (LDSP char*)smem;
  STAMP(0);
  bf16x8 Wo[16], Wd[56];
  unsigned xres[8];
  stage_qkv<0>(c, in, Wo, Wd, xres);
  relaunder(c);
  stage_proj<0, false, 16>(c, Wo, Wd, xres, out);
  relaunder(c);
  stage_gu<0>(c, Wd);
  relaunder(c);
  stage_proj<0, true, 56>(c, Wd, Wd, xres, out);
  relaunder(c);
  stage_qkv<1>(c, in, Wo, Wd, xres);
  relaunder(c);
  stage_proj<1, false, 16>(c, Wo, Wd, xres, out);
  relaunder(c);
  stage_gu<1>(c, Wd);
  relaunder(c);
  stage_proj<1, true, 56>(c, Wd, Wd, xres, out);
  if (blockIdx.x == 255) advance_epoch(c);
  asm volatile(".Lcode_end:");
}

extern "C" void kernel_launch(void* const* d_in, const int* in_sizes, int n_in, void* d_out, int out_size, void* d_ws,
                              size_t ws_size, hipStream_t stream) {
  Inputs in;
  in.x = (const __hip_bfloat16*)d_in[0];
  in.wqkv = (const __hip_bfloat16*)d_in[1];
  in.wo = (const __hip_bfloat16*)d_in[2];
  in.wg = (const __hip_bfloat16*)d_in[4];
  in.wu = (const __hip_bfloat16*)d_in[5];
  in.wd = (const __hip_bfloat16*)d_in[6];
  (void)hipFuncSetAttribute((const void*)main_kernel, hipFuncAttributeMaxDynamicSharedMemorySize, LDS_BYTES);
  main_kernel<<<256, 256, LDS_BYTES, stream>>>(in, (char*)d_ws, (__hip_bfloat16*)d_out);
}
```
